```python
import torch

def _softmax(x, axis=-1):
    return torch.softmax(x, dim=axis)
def _gelu(x):
    return torch.nn.functional.gelu(x, approximate="tanh")
def _silu(x):
    return torch.nn.functional.silu(x)
def _sigmoid(x):
    return torch.sigmoid(x)
def _layernorm(x, axis=-1, eps=1e-5):
    m = x.mean(dim=axis, keepdim=True)
    v = x.var(dim=axis, keepdim=True, unbiased=False)
    return (x - m) / torch.sqrt(v + eps)
def _scatter_add(tgt, idx, src, axis=0):
    return tgt.clone().index_add_(axis, idx, src)
def _segment_sum(src, seg, n):
    out = torch.zeros((n,) + src.shape[1:], dtype=src.dtype)
    return out.index_add_(0, seg, src)
def _pad(x, pw):
    flat = tuple(v for pair in reversed(pw) for v in pair)
    return torch.nn.functional.pad(x, flat)

kernel_name = 'parallel_sum__spectral_gfnet__glu_gated_reglu__prompt'

def setup_inputs():

    _w = (lambda a: (lambda t: t.to(torch.bfloat16) if t.is_floating_point() else t)(torch.as_tensor(a)))
    def _A_setup():
        B, S, D = 1, 2048, 448
        Df = D // 2 + 1
        return {
            "x": _w((lambda *s: torch.randn(*s, dtype=torch.bfloat16) * 0.05)(B, S, D)),
            "w_r": _w((lambda *s: torch.randn(*s, dtype=torch.bfloat16) * 0.4)(S, Df) * 0.02),
            "w_i": _w((lambda *s: torch.randn(*s, dtype=torch.bfloat16) * 0.4)(S, Df) * 0.02),
        }
    _A_in = _A_setup()

    def _B_setup():
        B, S, D, F = 1, 2048, 448, 1792
        return {
            "x": _w((lambda *s: torch.randn(*s, dtype=torch.bfloat16) * 0.05)(B, S, D)),
            "W_gate": _w((lambda *s: torch.randn(*s, dtype=torch.bfloat16) * 0.2)(D, F) * (2.0 / D) ** 0.5),
            "W_up": _w((lambda *s: torch.randn(*s, dtype=torch.bfloat16) * 0.2)(D, F) * (2.0 / D) ** 0.5),
            "W_down": _w((lambda *s: torch.randn(*s, dtype=torch.bfloat16) * 0.2)(F, D) * (1.0 / F) ** 0.5),
        }
    _B_in = _B_setup()

    return {
        "A_x": _A_in["x"],
        "A_w_r": _A_in["w_r"],
        "A_w_i": _A_in["w_i"],
        "B_x": _B_in["x"],
        "B_W_gate": _B_in["W_gate"],
        "B_W_up": _B_in["W_up"],
        "B_W_down": _B_in["W_down"],
    }

def reference(A_x, A_w_r, A_w_i, B_x, B_W_gate, B_W_up, B_W_down):
    def _A_ref(x, w_r, w_i):
        B, S, D = x.shape
        X = (lambda x, axes=(-2,-1): torch.fft.rfft2(x.float(), dim=axes))(x, axes=(-2, -1))
        W = w_r + 1j * w_i
        X = X * W
        y = (lambda x, s, axes=(-2,-1): torch.fft.irfft2(x, s=s, dim=axes).to(torch.bfloat16))(X, (S, D), axes=(-2, -1))
        return y

    def _B_ref(x, W_gate, W_up, W_down):
        gate = torch.einsum("bsd,df->bsf", x, W_gate)
        up = torch.einsum("bsd,df->bsf", x, W_up)
        gate = gate * (lambda x, dt: x.to(dt))(gate > 0, gate.dtype)
        h = gate * up
        return torch.einsum("bsf,fd->bsd", h, W_down)

    x = A_x
    xn = _layernorm(x, axis=-1)
    return x + _A_ref(xn, A_w_r, A_w_i) + _B_ref(xn, B_W_gate, B_W_up, B_W_down)
```

```cpp
#include <hip/hip_runtime.h>
#include <stdint.h>
#include <utility>

#define LDS __attribute__((address_space(3)))
#define GLB __attribute__((address_space(1)))
#define DI __device__ __forceinline__
#define UNROLL _Pragma("unroll")
typedef float f32x16 __attribute__((ext_vector_type(16)));
typedef float f32x4 __attribute__((ext_vector_type(4)));
typedef float f32x2 __attribute__((ext_vector_type(2)));
typedef unsigned u32x4 __attribute__((ext_vector_type(4)));
typedef unsigned u32x2 __attribute__((ext_vector_type(2)));
typedef __bf16 bf16x8 __attribute__((ext_vector_type(8)));
typedef __bf16 bf16x2 __attribute__((ext_vector_type(2)));
typedef short s16x4 __attribute__((ext_vector_type(4)));
typedef unsigned long long u64;
typedef __amdgpu_buffer_rsrc_t rsrc_t;

#if defined(__HIP_DEVICE_COMPILE__)
#pragma clang attribute push(__attribute__((target("no-packed-fp32-ops"))), apply_to = function)
#endif
#define TID __builtin_amdgcn_workitem_id_x()
#define BID __builtin_amdgcn_workgroup_id_x()

constexpr unsigned FS = 1024;
constexpr unsigned WS_E = 0, WS_XF = 1024, WS_ZF = 8192, WS_TF = 12288, WS_HF = 16384, WS_XD = 131072, WS_H = 2u << 20, WS_WT = 12u << 20;
constexpr unsigned WT_BYTES = 227u * 8192u, XD_BYTES = 448u * 4096u, NFG = 141u, H_BYTES = 64u * NFG * FS;
constexpr u64 TAG_K = 0x9E3779B97F4A7C15ull;
constexpr unsigned SLOT = 16384, FDB = 8 * SLOT, IFH = FDB + SLOT, MISC = FDB + 28672, LDS_BYTES = 163840;
constexpr unsigned MATS = 65536, HERM = 114688;
constexpr unsigned FULLA = MISC + 3328, C_FULL = FULLA + 248, C_EX = MISC, C_CX = MISC + 16, C_BN = MISC + 32, C_CS = MISC + 48, C_GZ = MISC + 64, C_GATE = MISC + 80, C_SD = MISC + 96,
                   C_FREE = MISC + 112, C_IF = MISC + 128, DUMP = MISC + 144, CSF = MISC + 512, CX = MISC + 512;
constexpr unsigned CODE_BYTES = 40832;
constexpr int PF_TOP = 5120, PF_FFT = 7552;
constexpr int PRE_X = 22, DEPTH = 22, PACE_D = 4;
constexpr int LW_AT = 72, LW_AT2 = 81, LW_AT3 = 99, LW_AT4 = 108;
constexpr int HSLEEP = 32, LPRIO = 1, HPRIO = 1, PPRIO = 0;
constexpr int TS = 4, E1W = 14, HA_AT = 171;
constexpr int PRE_H = 12, G2 = 55, B2 = 16, GZ = 76, BZ = 3;
constexpr int EXW = 16, EXR = 26;
constexpr int PSTART = 19, PS2 = 24, PSZ = 26;
constexpr unsigned PF_PART = (CODE_BYTES - PF_TOP) / 28;
constexpr int NOP = 28 * 9 + 56;
struct Stream { int n[NOP], i[NOP], c[NOP], d0; };
constexpr Stream mk_stream() {
  Stream s{}; int x = 0; bool in[29] = {};
  const int ord[] = {0, 1, 2, 3, 4, 5, 6, 7, 8, 9, 10, 11, 12, 13, 1428, 1529, 1630, 1731, 1832, 1933, 20, 2134, 22, 23, 24, 25, 26, 27};
  auto add = [&](int n, int i) { s.n[x] = n; s.i[x] = i; if (n > 20 && !s.d0) s.d0 = x; ++x; };
  for (int o : ord) {
    const int n = o >= 100 ? o / 100 : o;
    for (int i = 0; i < 8; ++i) { add(n, i); if (o >= 100) add(o % 100, i); }
    if (n < 28) { in[n] = true; int c = 0; while (in[c]) ++c; s.c[x] = c; add(n, 8); }
  }
  return s;
}
constexpr Stream ST = mk_stream();
constexpr unsigned tile_at(int n) { return n < 21 ? (n & 7) * SLOT : n == 21 || n == 30 ? FDB : ((n < 30 ? n - 1 : n - 2) & 7) * SLOT; }
constexpr int tile_need(int n) { return n < 8 ? 0 : n < 21 ? n - 7 : n == 21 ? 7 : n - 8; }
struct Sl { int ps, i, s; };
constexpr int SB0 = 224 - 16 * TS, R1 = SB0 + 8 * TS - 16;
constexpr Sl slot_of(int t) {
  const int r = t % 112;
  if (t < 16) return {t >> 2, 0, t & 3};
  if (t >= 80 && t < 112) return {(t - 80) >> 3, 5 + ((t >> 2) & 1), t & 3};
  if (t >= 112 && r < 32) return {r >> 3, (r >> 2) & 1, r & 3};
  if (t < SB0) return {r & 3, r >> 4, (r >> 2) & 3};
  const int b = (t - SB0) / (8 * TS) * 2, q = (t - SB0) % (8 * TS), Q = q - (8 * TS - 16);
  if (Q < 0) return {b + ((q >> 2) & 1), 7 - TS + (q >> 3), q & 3};
  return {b + (Q >> 3), 5 + ((Q >> 2) & 1), q & 3};
}
constexpr int full_need(int t) {
  if (t >= 224) return t < 336 && t % 8 == 0 ? 22 + (t - 224) / 8 : 0;
  if (t >= SB0) return t == R1 + 4 ? 21 : t == R1 - 8 ? 20 : t < R1 - 8 && t % 8 == 0 ? 22 - TS + (t - SB0) / 8 : 0;
  return t % 16 || t == 96 || t == 128 ? 0 : 8 + t / 16 + (t == 80 || t == 112);
}
constexpr int free_at(int t) {
  if (t >= 224) return t % 8 == 7 ? 22 + (t - 224) / 8 : 0;
  if (t >= SB0) return t == 223 ? 21 : t >= R1 + 16 && t < 208 && t % 8 == 7 ? 19 - (207 - t) / 8 : 0;
  return t % 16 != 15 || t == 95 || t == 127 ? 0 : 8 + t / 16;
}
constexpr int epi_done(int t) {
  if (t < 160) return t < 92 ? 0 : t < 135 ? (t - 91) * 28 / 11 : 112;
  if (t <= 216) return t < R1 + 12 ? 112 : t < R1 + 11 + 2 * E1W ? 112 + (t - R1 - 11) * 28 / E1W : 168;
  return 168 + (t - 216) * 4;
}
static_assert(TS >= 3 && TS <= 5 && E1W >= 8 && R1 + 11 + 2 * E1W <= 216, "");

constexpr int run(int t, int t0, int b, int n) { const int c = t < t0 ? 0 : t - t0 + b; return c < n ? c : n; }
constexpr int fetched(int t) { return run(t, -1, PRE_H, 28) + run(t, G2, B2, 28) + run(t, GZ, BZ, 15); }
static_assert(fetched(G2 - 2) == 28 && fetched(GZ - 2) == 56 && G2 + 28 - B2 >= 55 && HA_AT < R1 + 2, "");
struct alignas(128) Cnt { unsigned v[32]; };
static __device__ const Cnt CNT = {{1, 2, 3, 4, 5, 6, 7, 8, 9, 10, 11, 12, 13, 14, 15, 16, 17, 18, 19, 20, 21, 22, 23, 24, 25, 26, 27, 28}};

DI float asf(unsigned u) { return __builtin_bit_cast(float, u); }
DI unsigned asu(float f) { return __builtin_bit_cast(unsigned, f); }
DI LDS char* lp(unsigned off) { return (LDS char*)(size_t)off; }
DI u32x4 lds128(unsigned off) { return *(LDS u32x4*)lp(off); }
DI void st128(unsigned off, u32x4 v) { *(LDS u32x4*)lp(off) = v; }
DI u32x2 trd(unsigned off) { return __builtin_bit_cast(u32x2, __builtin_amdgcn_ds_read_tr16_b64_v4i16((LDS s16x4*)lp(off))); }
DI u32x4 trd2(unsigned off, unsigned d) { u32x2 a = trd(off), b = trd(off + d); return u32x4{a.x, a.y, b.x, b.y}; }
DI f32x16 mfma(u32x4 a, u32x4 b, f32x16 c) {
  return __builtin_amdgcn_mfma_f32_32x32x16_bf16(__builtin_bit_cast(bf16x8, a), __builtin_bit_cast(bf16x8, b), c, 0, 0, 0);
}
DI unsigned pk2(float a, float b) { return __builtin_bit_cast(unsigned, __builtin_convertvector(f32x2{a, b}, bf16x2)); }
DI float bfr(float a) { return asf(pk2(0.f, a)); }
DI f32x4 mfma16(u32x4 a, u32x4 b, f32x4 c) {
  return __builtin_amdgcn_mfma_f32_16x16x32_bf16(__builtin_bit_cast(bf16x8, a), __builtin_bit_cast(bf16x8, b), c, 0, 0, 0);
}
DI f32x16 mfma2(u32x2 a, u32x2 b, f32x16 c) {
  return __builtin_amdgcn_mfma_f32_32x32x8bf16_1k(__builtin_bit_cast(s16x4, a), __builtin_bit_cast(s16x4, b), c, 0, 0, 0);
}
DI float turn(float n, float o) { return __builtin_fmaf(__builtin_fmaf(-448.f, __builtin_floorf(n * (1.f / 448.f)), n), 1.f / 448.f, o); }
DI float relu(float v) { const int b = __builtin_bit_cast(int, v); return asf((unsigned)(b > 0 ? b : 0)); }
DI float halves_sum(float v) {
  u32x2 p = __builtin_amdgcn_permlane32_swap(asu(v), asu(v), false, false);
  return asf(p.x) + asf(p.y);
}
DI rsrc_t mkrsrc(const void* p, unsigned bytes) { return __builtin_amdgcn_make_buffer_rsrc((void*)p, 0, bytes, 0x00020000); }
#define SB() __builtin_amdgcn_sched_barrier(0)
template <int I> struct IC { static constexpr int v = I; };
template <int... I, class F> DI void sfor_(std::integer_sequence<int, I...>, F&& f) { (f(IC<I>{}), ...); }
template <int N, class F> DI void sfor(F&& f) { sfor_(std::make_integer_sequence<int, N>{}, f); }
#define INL __attribute__((always_inline))
DI unsigned rfl(unsigned v) { return __builtin_amdgcn_readfirstlane(v); }
DI void wg_barrier() { asm volatile("s_waitcnt lgkmcnt(0)\n\ts_barrier" ::: "memory"); }

template <unsigned OFF> DI void cset(unsigned cb, unsigned val) {
  asm volatile("ds_write_b32 %0, %1 offset:%2" :: "v"(cb), "v"(val), "n"(OFF - MISC) : "memory");
}
#define CW4(NAME, SL) \
  template <unsigned OFF> DI void NAME(unsigned cb4, unsigned need) { unsigned t, c; \
    asm volatile("s_mov_b32 %1, 0x80000\nLcw%=:\n\tds_read_b32 %0, %2 offset:%4\n\ts_waitcnt lgkmcnt(0)\n\tv_cmp_gt_u32 vcc, %3, %0\n\t" \
                 "s_cbranch_vccz Lcd%=\n\t" SL "s_sub_u32 %1, %1, 1\n\ts_cbranch_scc0 Lcw%=\nLcd%=:" \
                 : "=&v"(t), "=&s"(c) : "v"(cb4), "s"(need), "n"(OFF - MISC) : "memory", "scc", "vcc"); }
template <unsigned OFF, unsigned NEED> DI void cwait_i(unsigned cb4) { unsigned t, c;
  asm volatile("s_mov_b32 %1, 0x80000\nLcw%=:\n\tds_read_b32 %0, %2 offset:%4\n\ts_waitcnt lgkmcnt(0)\n\tv_cmp_gt_u32 vcc, %3, %0\n\t"
               "s_cbranch_vccz Lcd%=\n\ts_sub_u32 %1, %1, 1\n\ts_cbranch_scc0 Lcw%=\nLcd%=:"
               : "=&v"(t), "=&s"(c) : "v"(cb4), "n"(NEED), "n"(OFF - MISC) : "memory", "scc", "vcc"); }
CW4(cwaits, "s_sleep 1\n\t")
CW4(cwaitl, "s_sleep 4\n\t")
template <unsigned OFF> DI unsigned cpre_a(unsigned cb4) {
  unsigned t; asm volatile("ds_read_b32 %0, %1 offset:%2" : "=v"(t) : "v"(cb4), "n"(OFF - MISC) : "memory"); return t;
}
template <unsigned OFF> DI void cwait_a(unsigned cb4, unsigned need, unsigned t) { unsigned c;
  asm volatile("s_mov_b32 %1, 0x80000\nLcw%=:\n\ts_waitcnt lgkmcnt(0)\n\tv_cmp_gt_u32 vcc, %3, %0\n\ts_cbranch_vccz Lcd%=\n\ts_sleep 1\n\t"
               "ds_read_b32 %0, %2 offset:%4\n\ts_sub_u32 %1, %1, 1\n\ts_cbranch_scc0 Lcw%=\n\ts_waitcnt lgkmcnt(0)\nLcd%=:"
               : "+v"(t), "=&s"(c) : "v"(cb4), "s"(need), "n"(OFF - MISC) : "memory", "scc", "vcc");
}
template <unsigned OFF, unsigned NEED> DI void cwait_p(unsigned cb4, unsigned t) { unsigned c;
  asm volatile("s_mov_b32 %1, 0x80000\nLcw%=:\n\tv_cmp_gt_u32 vcc, %3, %0\n\ts_cbranch_vccz Lcd%=\n\t"
               "ds_read_b32 %0, %2 offset:%4\n\ts_sub_u32 %1, %1, 1\n\ts_waitcnt lgkmcnt(0)\n\ts_cbranch_scc0 Lcw%=\nLcd%=:"
               : "+v"(t), "=&s"(c) : "v"(cb4), "n"(NEED), "n"(OFF - MISC) : "memory", "scc", "vcc");
}
DI u64 gload64(const char* p) { return __hip_atomic_load((GLB u64*)p, __ATOMIC_RELAXED, __HIP_MEMORY_SCOPE_AGENT); }
DI void gstore64(char* p, u64 v) { __hip_atomic_store((GLB u64*)p, v, __ATOMIC_RELAXED, __HIP_MEMORY_SCOPE_AGENT); }
#define POLL "s_movk_i32 %[n], 0x7fff\nLpp%=:\n\tglobal_load_dwordx2 %[a], %[off], %[base] sc1\n\ts_waitcnt vmcnt(0)\n\tv_cmp_ne_u64 vcc, %[tag], %[a]\n\t" \
  "s_cbranch_vccz Lpq%=\n\ts_sleep 1\n\ts_sub_u32 %[n], %[n], 1\n\ts_cbranch_scc0 Lpp%=\nLpq%=:\n\t"
#define POLL_OUT [a] "=&v"(a_), [n] "=&s"(n_)
template <unsigned OFF> DI void poll_set(const char* base, unsigned off, u64 tag, unsigned cb, unsigned val) {
  u64 a_; unsigned n_;
  asm volatile(POLL "ds_write_b32 %[cb], %[val] offset:%[o]"
               : POLL_OUT : [off] "v"(off), [base] "s"(base), [tag] "v"(tag), [cb] "v"(cb), [val] "v"(val), [o] "n"(OFF - MISC) : "memory", "scc", "vcc");
}
DI void st_wt(u32x4 v, rsrc_t r, unsigned voff, unsigned soff) {
  __builtin_amdgcn_raw_buffer_store_b128(v, r, voff, soff, 16);
  asm volatile("s_nop 1" :: "v"(v) : "memory");
}
DI void st_flag(u64 tag, rsrc_t r, unsigned voff) {
  __builtin_amdgcn_raw_buffer_store_b64(__builtin_bit_cast(u32x2, tag), r, voff, 0, 16);
}
DI void touch(const void* p, unsigned bytes, unsigned voff) {
  __builtin_amdgcn_raw_ptr_buffer_load_lds(mkrsrc(p, bytes), (LDS void*)lp(MISC + 3840), 4, voff, 0, 0, 0);
}
DI const char* here() { u64 pc; asm volatile("s_getpc_b64 %0" : "=s"(pc)); return (const char*)pc; }
DI void drain() { asm volatile("s_waitcnt vmcnt(0)" ::: "memory"); }

DI void pack8(const f32x16& a, u32x4& lo, u32x4& hi) {
  UNROLL for (int i = 0; i < 4; ++i) { lo[i] = pk2(a[2 * i], a[2 * i + 1]); hi[i] = pk2(a[8 + 2 * i], a[9 + 2 * i]); }
}

DI void fft_wg(int g, const char* wr, const char* wi, char* ws) {
  touch(here(), PF_FFT, TID * 128);
  SB();
  const int lane = TID & 63, w = rfl(TID >> 6), L = lane & 31, h = lane >> 5;
  const unsigned l16 = lane * 16;
  {
    const rsrc_t rw = mkrsrc((w < 4 ? wr : wi) + (16 * (g & 1) + 32 * (8 * ((g >> 2) & 3) + 4 * ((g >> 1) & 1) + (w & 3)) + 1024 * (g >> 4)) * 450, 7200);
    sfor<8>([&](auto I) INL {
      __builtin_amdgcn_raw_ptr_buffer_load_lds(rw, (LDS void*)lp(w * 8192 + (I.v >> 2) * 4096), 16, l16 + (I.v >> 2) * 4096, 0, (I.v & 3) * 1024, 2);
    });
  }
  u64 tag = gload64(ws + WS_E);
  SB();
#pragma unroll 1
  for (int job = w; job < 10; job += 8) {
    const int mt = job >> 1;
    const float sl = mt == 4 ? (float)L * (1.f / 1024.f) : (float)L * (1.f / 32.f) + (mt == 2 ? 1.f / 64.f : mt == 3 ? 1.f / 2048.f : 0.f);
    const float p0 = (float)(16 * (job & 1) + 4 * h) * sl + (mt == 1 ? (float)L * (1.f / 64.f) : 0.f);
    float c[8], sn[8];
    UNROLL for (int j = 0; j < 8; ++j) {
      const float ph = __builtin_fmaf((float)(8 * (j >> 2) + (j & 3)), sl, p0);
      c[j] = __builtin_amdgcn_cosf(ph); sn[j] = __builtin_amdgcn_sinf(ph);
    }
    const unsigned a = MATS + job * 4096 + l16;
    if (job < 8) {
      const u32x4 n = {pk2(sn[0], sn[1]), pk2(sn[2], sn[3]), pk2(sn[4], sn[5]), pk2(sn[6], sn[7])};
      st128(a, u32x4{pk2(c[0], c[1]), pk2(c[2], c[3]), pk2(c[4], c[5]), pk2(c[6], c[7])});
      st128(a + 1024, n);
      st128(a + 2048, n ^ 0x80008000u);
    } else {
      UNROLL for (int q = 0; q < 2; ++q) {
        st128(a + q * 1024, u32x4{asu(c[4 * q]), asu(c[4 * q + 1]), asu(c[4 * q + 2]), asu(c[4 * q + 3])});
        st128(a + 2048 + q * 1024, u32x4{asu(sn[4 * q]), asu(sn[4 * q + 1]), asu(sn[4 * q + 2]), asu(sn[4 * q + 3])});
      }
      *(LDS unsigned*)lp(MISC + 4 * lane) = 0;
    }
  }
  SB();
  const rsrc_t rwt = mkrsrc(ws + WS_WT, WT_BYTES);
  asm volatile("s_waitcnt vmcnt(1)" ::: "memory");
  wg_barrier();
  {
    const int odd = (lane >> 3) & 1, ch = min(4 * w + (lane >> 4), 28);
    const unsigned la = (lane & 7) * 900 + odd * 448 + ch * 16, ga = (8 * ch + 1 - odd) * 8192 + (lane & 15) * 16;
#pragma unroll 1
    for (int i = w == 7 ? 0 : 3; i >= 0; --i) {
      unsigned re[4], im[4];
      UNROLL for (int e = 0; e < 4; ++e) { re[e] = *(LDS unsigned*)lp(la + 4 * i + e * 8192); im[e] = *(LDS unsigned*)lp(la + 4 * i + e * 8192 + 32768); }
      UNROLL for (int k = 0; k < 2; ++k) {
        u32x4 o;
        UNROLL for (int e = 0; e < 4; ++e) o[e] = __builtin_amdgcn_perm(im[e], re[e], k ? 0x07060302u : 0x05040100u);
        st_wt(o, rwt, ga, (2 * i + k) * 8192 + g * 256);
      }
    }
  }
  tag += TAG_K;
  struct Mat { u32x4 r[2], i[2], n[2]; } F, P;
  unsigned ma = MATS + l16; asm volatile("" : "+v"(ma));
  auto mat = [&](int k, Mat& M) INL {
    UNROLL for (int s = 0; s < 2; ++s) { M.r[s] = lds128(ma + k * 8192 + s * 4096); M.n[s] = lds128(ma + k * 8192 + s * 4096 + 1024); M.i[s] = lds128(ma + k * 8192 + s * 4096 + 2048); }
  };
  mat(0, F); mat(1, P);
  rsrc_t rf = mkrsrc(ws + WS_TF + w * 256 + g * 8, 8);
  asm volatile("" : "+s"(rf), "+v"(tag));
  drain();
  st_flag(tag, rf, l16);
  if (g >= 29) return;
  UNROLL for (int s = 0; s < 2; ++s) asm volatile("" : "+v"(F.r[s]), "+v"(F.i[s]), "+v"(F.n[s]), "+v"(P.r[s]), "+v"(P.i[s]), "+v"(P.n[s]));
  if (w == 7) poll_set<DUMP>(ws + WS_TF, 8 * L + ((g + h) >> 2) * 256, tag, MISC, 0);
  wg_barrier();
  const unsigned own = w * 8192 + l16;
  {
    f32x16 wre[2], wim[2];
    u32x4 ar[2][2], ai[2][2];
    u32x4 wv[8];
#define XLD(r, v, o) "buffer_load_dwordx4 %[" r "], %[" v "], %[rx], 0 offen offset:" o " sc1\n\t"
#define XLD8 XLD("r0", "va", "0") XLD("r1", "va", "1024") XLD("r2", "va", "2048") XLD("r3", "va", "3072") \
             XLD("r4", "vb", "0") XLD("r5", "vb", "1024") XLD("r6", "vb", "2048") XLD("r7", "vb", "3072")
    {
      const unsigned wa = (lane & 48) * 16 + (lane & 1) * 128 + (lane & 14) * 8 + min(8 * g + w + 1, 225) * 8192;
      asm volatile(XLD8 : [r0] "=&v"(wv[0]), [r1] "=&v"(wv[1]), [r2] "=&v"(wv[2]), [r3] "=&v"(wv[3]), [r4] "=&v"(wv[4]), [r5] "=&v"(wv[5]), [r6] "=&v"(wv[6]), [r7] "=&v"(wv[7])
                   : [va] "v"(wa), [vb] "v"(wa + 4096), [rx] "s"(rwt) : "memory");
    }
    {
      u64 a_; unsigned n_;
      const unsigned xa = l16 + (g < 28 ? 8 * g + w : 224 * w) * 8192;
      asm volatile("s_cmp_lg_u32 %[w], 7\n\ts_cbranch_scc1 Lpq%=\n\t" POLL "s_barrier\n\t" XLD8
                   : POLL_OUT, [r0] "=&v"(ar[0][0]), [r2] "=&v"(ar[0][1]), [r4] "=&v"(ar[1][0]), [r6] "=&v"(ar[1][1]),
                     [r1] "=&v"(ai[0][0]), [r3] "=&v"(ai[0][1]), [r5] "=&v"(ai[1][0]), [r7] "=&v"(ai[1][1])
                   : [off] "v"(8 * L), [base] "s"(ws + WS_XF + (g < 28 ? (g >> 1) : 0) * 256), [tag] "v"(tag), [w] "s"(w),
                     [va] "v"(xa), [vb] "v"(xa + 4096), [rx] "s"(mkrsrc(ws + WS_XD, XD_BYTES))
                   : "memory", "scc", "vcc");
      asm volatile("s_waitcnt vmcnt(8)" : "+v"(wv[0]), "+v"(wv[1]), "+v"(wv[2]), "+v"(wv[3]), "+v"(wv[4]), "+v"(wv[5]), "+v"(wv[6]), "+v"(wv[7]));
      SB();
    }
    UNROLL for (int i = 0; i < 32; ++i) { wre[i >> 4][i & 15] = asf(wv[i >> 2][i & 3] << 16); wim[i >> 4][i & 15] = asf(wv[i >> 2][i & 3] & 0xffff0000u); }
    if (w == 0 && (g == 0 || g == 28)) {
      unsigned ha = HERM + 4 * L + 512 * h, hr = HERM + 8192 - 7552 - 4 * L - 512 * h;
      asm volatile("" : "+v"(ha), "+v"(hr));
      UNROLL for (int i = 0; i < 32; ++i) *(LDS unsigned*)lp(ha + 1024 * (i >> 2) + 128 * (i & 3)) = wv[i >> 2][i & 3];
      *(LDS unsigned*)lp(ha + 8192) = wv[0][0];
      SB();
      UNROLL for (int i = 0; i < 32; ++i) wv[i >> 2][i & 3] = *(LDS unsigned*)lp(hr + 7552 - 1024 * (i >> 2) - 128 * (i & 3));
      SB();
      UNROLL for (int i = 0; i < 32; ++i) { wre[i >> 4][i & 15] += asf(wv[i >> 2][i & 3] << 16); wim[i >> 4][i & 15] -= asf(wv[i >> 2][i & 3] & 0xffff0000u); }
    }
    asm volatile("" : "+v"(wre[0]), "+v"(wre[1]), "+v"(wim[0]), "+v"(wim[1]));
    SB();
    {
      asm volatile("s_waitcnt vmcnt(0)" : "+v"(ar[0][0]), "+v"(ar[0][1]), "+v"(ar[1][0]), "+v"(ar[1][1]), "+v"(ai[0][0]), "+v"(ai[0][1]), "+v"(ai[1][0]), "+v"(ai[1][1]));
      UNROLL for (int i = 0; i < 8; ++i) {
        unsigned u = ar[i >> 2][(i >> 1) & 1][2 + (i & 1)], v = ai[i >> 2][(i >> 1) & 1][i & 1];
        asm volatile("v_swap_b32 %0, %1" : "+v"(u), "+v"(v));
        ar[i >> 2][(i >> 1) & 1][2 + (i & 1)] = u; ai[i >> 2][(i >> 1) & 1][i & 1] = v;
      }
    }
    f32x16 z16; UNROLL for (int i = 0; i < 16; ++i) z16[i] = 0.f;
    f32x16 r0, i0, r1, i1;
    auto md = [&](int t, const Mat& M, f32x16& r, f32x16& i, const f32x16& cr, const f32x16& ci) INL {
      r = mfma(M.r[0], ar[t][0], cr); i = mfma(M.i[0], ar[t][0], ci); r = mfma(M.n[0], ai[t][0], r); i = mfma(M.r[0], ai[t][0], i);
      r = mfma(M.r[1], ar[t][1], r); i = mfma(M.i[1], ar[t][1], i); r = mfma(M.n[1], ai[t][1], r); i = mfma(M.r[1], ai[t][1], i);
    };
    auto dm = [&](int t, const Mat& M, f32x16& r, f32x16& i) INL {
      r = mfma(ar[t][0], M.r[0], z16); i = mfma(ar[t][0], M.n[0], z16); r = mfma(ai[t][0], M.i[0], r); i = mfma(ai[t][0], M.r[0], i);
      r = mfma(ar[t][1], M.r[1], r); i = mfma(ar[t][1], M.n[1], i); r = mfma(ai[t][1], M.i[1], r); i = mfma(ai[t][1], M.r[1], i);
    };
    md(0, F, r0, i0, z16, z16);
    md(1, P, r1, i1, r0, i0);
    SB();
    mat(2, P);
    r0 = 2.f * r0 - r1; i0 = 2.f * i0 - i1;
    {
      const f32x16 yr = r1 * wre[0] - i1 * wim[0], yi = r1 * wim[0] + i1 * wre[0];
      r1 = yr + r0 * wre[1] - i0 * wim[1]; i1 = yi + r0 * wim[1] + i0 * wre[1];
      pack8(r1, ar[0][0], ar[0][1]); pack8(i1, ai[0][0], ai[0][1]);
      pack8(2.f * yr - r1, ar[1][0], ar[1][1]); pack8(2.f * yi - i1, ai[1][0], ai[1][1]);
    }
    f32x16 tc, ts;
    UNROLL for (int q = 0; q < 4; ++q) {
      const u32x4 c = lds128(ma + 32768 + (q >> 1) * 4096 + (q & 1) * 1024), sn = lds128(ma + 34816 + (q >> 1) * 4096 + (q & 1) * 1024);
      UNROLL for (int e = 0; e < 4; ++e) { tc[4 * q + e] = asf(c[e]); ts[4 * q + e] = asf(sn[e]); }
    }
    dm(0, F, r0, i0);
    dm(1, P, r1, i1);
    Mat H; mat(3, H);
    pack8(r0 * tc - i0 * ts, ar[0][0], ar[0][1]); pack8(i0 * tc + r0 * ts, ai[0][0], ai[0][1]);
    pack8(r1 * tc - i1 * ts, ar[1][0], ar[1][1]); pack8(i1 * tc + r1 * ts, ai[1][0], ai[1][1]);
    dm(0, F, r0, i0);
    dm(1, H, r1, i1);
    UNROLL for (int t = 0; t < 2; ++t) UNROLL for (int q = 0; q < 4; ++q) {
      u32x4 a;
      UNROLL for (int e = 0; e < 4; ++e) a[e] = t ? pk2(r1[4 * q + e], i1[4 * q + e]) : pk2(r0[4 * q + e], i0[4 * q + e]);
      st128(own + t * 4096 + q * 1024, a);
    }
  }
  {
    const int hh = w >> 2, t = (w >> 1) & 1;
    cset<MISC>(lane ? DUMP : MISC + 4 * w, 1);
    cwait_i<MISC, 1>(MISC + 16 * hh + 4 * (lane & 3));
    const rsrc_t rh = mkrsrc(ws + WS_H, H_BYTES);
    UNROLL for (int qq = 0; qq < 2; ++qq) {
      const int q = 2 * (w & 1) + qq;
      u32x4 v[4];
      UNROLL for (int i = 0; i < 4; ++i) v[i] = lds128((4 * hh + i) * 8192 + t * 4096 + q * 1024 + l16);
      UNROLL for (int e = 0; e < 4; ++e)
        st_wt(u32x4{v[0][e], v[1][e], v[2][e], v[3][e]}, rh, h * (4 * NFG * FS) + L * 16, ((32 * t + 8 * q + e) * NFG + 112 + g) * FS + hh * (FS / 2));
    }
  }
  drain();
  unsigned done;
  asm volatile("ds_add_rtn_u32 %0, %1, %2 offset:32\n\ts_waitcnt lgkmcnt(0)" : "=v"(done) : "v"(MISC + 4 * lane), "v"(1u) : "memory");
  st_flag(tag, mkrsrc(ws + WS_ZF + g * 8, rfl(done) == 7 ? 2048 : 0), lane * 256);
}

__global__ __launch_bounds__(512, 2) void fused_kernel(const char* __restrict__ x, const char* __restrict__ wr, const char* __restrict__ wi,
                                                       const char* __restrict__ wg, const char* __restrict__ wu, const char* __restrict__ wd,
                                                       char* __restrict__ out, char* __restrict__ ws) {
  const int b = BID, xcd = b & 7, slot = b >> 3;
  if (slot >= 28) {
    asm volatile("" :: "s"(x), "s"(wg), "s"(wu), "s"(wd), "s"(out));
    const int g = 4 * xcd + (slot - 28);
    fft_wg(g, wr, wi, ws);
    return;
  }
  touch(here(), PF_TOP + PF_PART * (slot + 1), TID * 128 + (TID >= PF_TOP / 128 ? PF_PART * slot : 0));
  SB();
  int lane = TID & 63, w = rfl(TID >> 6);
  *(volatile LDS unsigned*)lp(MISC + 4 * lane + (w >> 2) * 3456) = 0;
  SB();
  const int a = (slot * 37) >> 8, c7 = slot - 7 * a;
  const int p = 4 * (xcd >> 1) + a, j = 7 * (xcd & 1) + c7;
  const int rt = 4 * xcd + a;
  const int rot = c7;
  const int lw = w & 1;
  auto xoff = [&](int i) { return ((57344 * (lane >> 3) + 16 * ((lane & 7) ^ (lane >> 4))) ^ (64 * (i & 1))) + (8 * 57344 * (i & 3) + 1792 * (i >> 2) - (i & 3) * 1024 + 3072); };
  auto kblk = [&](int e) { int k = rot + e; return e && k >= 7 ? k - 7 : k; };
  auto dma = [&](rsrc_t r, unsigned voff, unsigned soff, auto N, auto I) INL {
    __builtin_amdgcn_raw_ptr_buffer_load_lds(r, (LDS void*)lp(tile_at(N.v) + 8192 * lw + (I.v >> 2) * 4096), 16, voff, soff, (I.v & 3) * 1024, 0);
  };
  const rsrc_t rsi = mkrsrc(&CNT, sizeof(Cnt));
  auto flag = [&](int n) INL { __builtin_amdgcn_raw_ptr_buffer_load_lds(rsi, (LDS void*)lp(FULLA + 256 * lw), 4, 0, 4 * n, 0, 0); };
  int xo = 896 * (8 * (p & 7) + (p >> 3) + 4 * lw), x0 = xo + rot * 128;
  unsigned v0 = xoff(0);
  asm volatile("" : "+s"(x), "+s"(xo), "+s"(x0), "+v"(v0), "+s"(w) : "s"(wr), "s"(wi));
  rsrc_t rsx = mkrsrc(x - 3072, 2048 * 896 + 3072);
  asm volatile("s_waitcnt lgkmcnt(0)\n\ts_barrier" : "+s"(rsx) :: "memory");
  if (__builtin_expect((w >> 1) == 2, 1)) {
    unsigned vo[8];
    sfor<PRE_X>([&](auto X) INL {
      constexpr int n = X.v / 9, i = X.v % 9;
      if (X.v < 8) vo[i & 7] = (v0 ^ (64 * (i & 1))) + (8 * 57344 * (i & 3) + 1792 * (i >> 2) - (i & 3) * 1024);
      if (i == 8) flag(n); else dma(rsx, vo[i & 7], n ? xo + kblk(n) * 128 : x0, IC<n>{}, IC<i>{});
      SB();
    });
  }
  asm volatile("" : "+s"(w), "+v"(lane));
  if (w >= 6) __builtin_amdgcn_s_sleep(HSLEEP);
  asm volatile("" : "+s"(w), "+v"(lane));
  const int m = lane & 31, h = lane >> 5;
  const unsigned cb4 = MISC + 4 * (lane & 3);
  const unsigned cbw = 4 * lane + (lane < (w < 4 ? 1 : 2) ? MISC + (w < 4 ? 4 * w : 8 * (w & 1)) : DUMP);
  u64 tag = 0;
  if (__builtin_expect((w >> 1) == 2, 0)) {
    const int r8 = lane >> 3, pc = lane & 7, b4 = (lane >> 4) & 1;
    unsigned vo[8];
    UNROLL for (int i = 0; i < 8; ++i) vo[i] = xoff(i);
    __builtin_amdgcn_s_setprio(LPRIO);
    const rsrc_t rsw = mkrsrc(lw ? wu : wg, 448 * 3584), rsd = mkrsrc(wd, 1792 * 896);
    unsigned vd[8], fr = 0, la = lane * 16;
    u32x4 rg[56];
    sfor<NOP - PRE_X>([&](auto XX) INL {
      constexpr int X = XX.v + PRE_X, n = ST.n[X], i = ST.i[X];
      SB();
      if (n == 7 && i == 0) UNROLL for (int e = 0; e < 8; ++e) vo[e] = (8 * e + r8) * 3584 + 32 * ((pc >> 1) ^ (b4 | (lw << 1))) + 16 * (pc & 1) - (e & 3) * 1024;
      if (X == ST.d0) UNROLL for (int e = 0; e < 8; ++e) vd[e] = (8 * e + r8) * 896 + 16 * (pc ^ (b4 << 2)) - (e & 3) * 1024;
      if (i == 0 && n >= 8 && n < 28) {
        if (n == 14) cwaits<C_SD>(cb4, 1);
        cwait_a<C_FREE>(cb4, tile_need(n), fr);
      }
      if (X == LW_AT) touch((lw ? wu : wg) + (unsigned)(28 * 3584 * p + 256 * j), 27 * 3584 + 256, (lane >> 1) * 3584 + (lane & 1) * 128);
      if (X == LW_AT2) touch(wd + (unsigned)(28 * 896 * (2 * rt + lw) + 128 * c7), 27 * 896 + 128, lane * 896);
      if (X == LW_AT3) touch((lw ? wu : wg) + (unsigned)(28 * 3584 * (p ^ 4) + 256 * j + 128), 27 * 3584 + 128, lane * 3584);
      if (X == LW_AT4) touch(wd + (unsigned)(28 * 896 * (2 * (rt ^ 4) + lw) + 128 * c7), 27 * 896 + 128, lane * 896);
      if (X >= DEPTH) asm volatile("s_waitcnt vmcnt(%0)" :: "n"(DEPTH - 1) : "memory");
      if (n >= 22 && n < 28 && PACE_D) asm volatile("s_nop %0" :: "n"(PACE_D - 1));
      if (i == 8) flag(ST.c[X] - 1);
      else if (n < 7) dma(rsx, vo[i & 7], xo + kblk(n) * 128, IC<n>{}, IC<i>{});
      else if (n < 21) dma(rsw, vo[i & 7], kblk(n % 7) * 64 * 3584 + 256 * j + 128 * (n >= 14), IC<n>{}, IC<i>{});
      else if (n < 28) dma(rsd, vd[i & 7], (kblk(n - 21) * 256 + 128 * lw) * 896 + 128 * c7, IC<n>{}, IC<i>{});
      else rg[(8 * n + i) % 56] = __builtin_amdgcn_raw_buffer_load_b128(rsd, vd[i & 7] + (i & 3) * 1024, (kblk(n - 28) * 256 + 128 * lw + 64) * 896 + 128 * c7, 0);
      if constexpr (X + 4 < NOP) if (ST.i[X + 4] == 0 && ST.n[X + 4] >= 8 && ST.n[X + 4] < 28) fr = cpre_a<C_FREE>(cb4);
    });
    asm volatile("s_waitcnt vmcnt(0)" ::: "memory");
    la += 8192 * lw;
    sfor<7>([&](auto K) INL {
      constexpr int n = 28 + K.v;
      unsigned a = la + tile_at(n); asm volatile("" : "+v"(a));
      cwaits<C_FREE>(cb4, tile_need(n));
      UNROLL for (int i = 0; i < 8; ++i) st128(a + i * 1024, rg[8 * K.v + i]);
      cset<C_FULL>(cbw, n + 1);
    });
  } else {
    tag = gload64(ws + WS_E);
    if (__builtin_expect(w < 4, 1)) {
      const int mi = w;
      {
        const int u = 16 * j + (m >> 1), part = m & 1;
        const float uf = (u == 0 && part) ? 224.f : (float)u, step = uf * (1.f / 448.f), off = (u != 0 && part) ? 0.25f : 0.f;
        const float s64 = turn(64.f * uf, 0.f), pb = turn((float)(64 * rot + 16 * w + 8 * h) * uf, off);
        const float sc = u == 0 ? 0.5f / 917504.f : 2.f / 917504.f, k2 = 2.f * __builtin_amdgcn_cosf(step), k64 = 2.f * __builtin_amdgcn_cosf(s64);
        f32x16 cs; UNROLL for (int i = 0; i < 16; ++i) cs[i] = 0.f;
        float c[7][8];
        UNROLL for (int i = 0; i < 2; ++i) {
          const float p0 = i ? pb + s64 : pb;
          c[i][0] = sc * __builtin_amdgcn_cosf(p0); c[i][1] = sc * __builtin_amdgcn_cosf(p0 + step);
        }
        UNROLL for (int e = 2; e < 8; ++e) UNROLL for (int i = 0; i < 2; ++i) c[i][e] = __builtin_fmaf(k2, c[i][e - 1], -c[i][e - 2]);
        UNROLL for (int i = 0; i < 7; ++i) {
          if (i >= 2) UNROLL for (int e = 0; e < 8; ++e) c[i][e] = __builtin_fmaf(k64, c[i - 1][e], -c[i - 2][e]);
          const u32x4 o = {pk2(c[i][0], c[i][1]), pk2(c[i][2], c[i][3]), pk2(c[i][4], c[i][5]), pk2(c[i][6], c[i][7])};
          st128(FDB + (4 * i + w) * 1024 + lane * 16, o);
          cs = mfma(u32x4{0x3f803f80u, 0x3f803f80u, 0x3f803f80u, 0x3f803f80u}, o, cs);
        }
        *(LDS float*)lp(CSF + m * 16 + w * 4) = cs[0];
      }
      __builtin_amdgcn_s_setprio(2);
      cset<C_CS>(cbw, 1);
      u32x4 yc[2], ys[2];
      {
        const float sl = (float)m * (1.f / 32.f), p0 = (float)(m * (256 * h + 8 * (p & 7) + (p >> 3) + 2 * mi)) * (1.f / 2048.f);
        UNROLL for (int i = 0; i < 8; ++i) {
          const float a0 = __builtin_fmaf((float)(8 * (i >> 1) + 2 * (i & 1)), sl, p0), a1 = a0 + sl;
          yc[i >> 2][i & 3] = pk2(__builtin_amdgcn_cosf(a0), __builtin_amdgcn_cosf(a1));
          ys[i >> 2][i & 3] = pk2(__builtin_amdgcn_sinf(a0), __builtin_amdgcn_sinf(a1));
        }
        asm volatile("" : "+v"(yc[0]), "+v"(yc[1]), "+v"(ys[0]), "+v"(ys[1]));
      }
      cwait_i<C_CS, 1>(cb4);
      unsigned fw; u64 late;
      unsigned cf = cb4; asm volatile("" : "+v"(cf));
#define cb4 cf
      auto full_pre = [&]() { fw = *(volatile LDS unsigned*)lp(cf + (C_FULL - MISC)); };
      auto full_cmp = [&](unsigned need) { late = __builtin_amdgcn_ballot_w64(fw < need); };
      auto full_wait = [&](auto N) INL { if (__builtin_expect(late != 0, 0)) cwait_i<C_FULL, N.v>(cb4); };
      f32x16 z16; UNROLL for (int i = 0; i < 16; ++i) z16[i] = 0.f;
      const f32x4 z4 = {0.f, 0.f, 0.f, 0.f};
      const unsigned sel1 = (((lane >> 2) ^ (lane >> 4)) & 1) ? 0u : 0x3f803f80u;
      const u32x4 sel = {sel1, sel1, sel1, sel1};
      auto split2 = [&](float v) { const float vh = bfr(v); return pk2(vh, v - vh); };
      rsrc_t rh;
      int so[4];
      const int blk = (lane >> 4) & 1, q4 = (lane >> 2) & 3, p4 = lane & 3;
      unsigned l16 = lane * 16; asm volatile("" : "+v"(l16));
      u64 tg;
      const char* fp; rsrc_t rf;
      constexpr int LA = 6;
      u32x4 xf[28], wf[LA];
      f32x16 acc1, rb;
      u32x2 mn;
      float rr2; unsigned xs, ysg;
      unsigned wa[2][4];
      auto rdw = [&](auto T) INL {
        constexpr Sl o = slot_of(T.v);
        constexpr int sl = (7 + 7 * (T.v / 112) + o.i) & 7;
        wf[T.v % LA] = trd2(wa[sl >> 2][o.ps] + (sl & 3) * SLOT + 2048 * o.s, 512);
      };
      {
        constexpr int DEF = 20;
        unsigned xa[4];
        UNROLL for (int s = 0; s < 4; ++s) { xa[s] = (32 * mi + m) * 128 + 16 * ((2 * s + h) ^ ((m >> 1) & 7)); asm volatile("" : "+v"(xa[s])); }
        unsigned fa = FDB + l16; asm volatile("" : "+v"(fa));
        u32x4 fd[4];
        f32x16 gram; f32x4 s1v;
        auto rdx = [&](int u) {
          if (u == 16) UNROLL for (int s = 0; s < 4; ++s) { xa[s] += 4 * SLOT; asm volatile("" : "+v"(xa[s])); }
          xf[u] = lds128(xa[u & 3] + ((u >> 2) & 3) * SLOT);
        };
        u32x4 csp; u32x2 csf;
        u32x2 one2; u64 k1, k2, k4, k8, k16;
        UNROLL for (int u = 0; u < 3; ++u) fd[u] = lds128(fa + u * 1024);
        cwait_i<C_FULL, 1>(cb4);
        UNROLL for (int u = 0; u < 3; ++u) rdx(u);
        full_pre();
        sfor<28>([&](auto U) INL {
          constexpr int u = U.v;
          SB();
          gram = mfma(xf[u], xf[u], u ? gram : z16);
          asm volatile("" : "+v"(gram));
          SB();
          if (u + 3 < 28) {
            if ((u & 3) == 1) cwait_p<C_FULL, (u >> 2) + 2>(cb4, fw);
            rdx(u + 3);
            if (u <= DEF) fd[(u + 3) & 3] = lds128(fa + (u + 3) * 1024);
            if (u == 0 || u == 8 || u == 16) cset<C_FREE>(cbw, (u >> 2) + 1);
          }
          SB();
          s1v = mfma16(sel, xf[u], u ? s1v : z4);
          if (u < DEF) acc1 = mfma(xf[u], fd[u & 3], u ? acc1 : z16);
          asm volatile("" : "+v"(s1v), "+v"(acc1));
          SB();
          if ((u & 3) == 3 && u < 20) full_pre();
          if (u >= 2 && u < 6) {
            constexpr int ps = (u - 2) & 3;
            wa[0][ps] = 8192 * blk + 128 * (8 * h + q4) + 32 * (((ps + mi) & 3) ^ ((q4 >> 1) | (blk << 1))) + 8 * p4;
            wa[1][ps] = wa[0][ps] + 4 * SLOT;
            asm volatile("" : "+v"(wa[0][ps]), "+v"(wa[1][ps]));
          }
          if (u >= 6 && u < 10) { constexpr int e = (u - 6) & 3; so[e] = ((4 * p + mi) * NFG + 8 * j + ((e + mi) & 3)) * FS; asm volatile("" : "+s"(so[e])); }
          if (u == 10) { rh = mkrsrc(ws + WS_H, H_BYTES); asm volatile("" : "+s"(rh)); }
          if (u == 11) { k1 = 0xAAAAAAAAAAAAAAAAull; k2 = 0xCCCCCCCCCCCCCCCCull; k8 = 0xFF00FF00FF00FF00ull; asm volatile("" : "+s"(k1), "+s"(k2), "+s"(k8)); }
          if (u == 12) { k16 = 0xFFFF0000FFFF0000ull; k4 = 0xF0F0F0F00F0F0F0Full; asm volatile("" : "+s"(k16), "+s"(k4)); }
          if (u == 13) { ysg = (l16 & 16) * 0x08000800u; asm volatile("" : "+v"(ysg)); }
          if (u == 14) { one2 = u32x2{h ? 0u : 0x3f803f80u, 0}; asm volatile("" : "+v"(one2)); }
          if (u == 22) csp = lds128(CSF + m * 16);
          if (u == 25) { csf = u32x2{split2((asf(csp[0]) + asf(csp[1])) + (asf(csp[2]) + asf(csp[3]))), 0}; asm volatile("" : "+v"(csf)); }
        });
        auto pick = [](float a, float b, u64 k) { return __builtin_amdgcn_inverse_ballot_w64(k) ? b : a; };
        float gd[8], mean;
        sfor<28 - DEF>([&](auto D) INL {
          constexpr int d = D.v, u = DEF + d;
          SB();
          acc1 = mfma(xf[u], fd[u & 3], acc1);
          asm volatile("" : "+v"(acc1));
          SB();
          if (u + 4 < 28) fd[u & 3] = lds128(fa + (u + 4) * 1024);
          if (d == 0) {
            asm volatile("" : "+v"(tag)); tg = (((u64)rfl(tag >> 32) << 32) | rfl(tag)) + TAG_K;
            xs = 6 * SLOT + mi * 4096 + l16;
            asm volatile("" : "+v"(xs), "+s"(tg));
            SB();
            mean = s1v[0] * (-1.f / 448.f); asm volatile("" : "+v"(mean));
          }
          if (d == 1) { UNROLL for (int e = 0; e < 5; ++e) gd[e] = pick(gram[2 * e], gram[2 * e + 1], k1); }
          if (d == 2) { UNROLL for (int e = 5; e < 8; ++e) gd[e] = pick(gram[2 * e], gram[2 * e + 1], k1); gd[0] = pick(gd[0], gd[1], k2); gd[2] = pick(gd[2], gd[3], k2); }
          if (d == 3) {
            cset<C_FREE>(cbw, 7);
            gd[4] = pick(gd[4], gd[5], k2); gd[6] = pick(gd[6], gd[7], k2); gd[0] = pick(gd[0], gd[2], k8);
          }
          if (d == 4) { full_pre(); gd[4] = pick(gd[4], gd[6], k8); gd[0] = pick(0.f, pick(gd[0], gd[4], k16), k4); gd[0] = halves_sum(gd[0]); }
          if (d == 5) gd[0] = bfr(bfr(__builtin_fmaxf(__builtin_fmaf(-mean, mean, gd[0] * (1.f / 448.f)), 0.f)) + 1e-5f);
          if (d == 6) { gd[0] = __builtin_amdgcn_rcpf(bfr(__builtin_amdgcn_sqrtf(gd[0]))); full_cmp(8); }
          if (d == 7) { rr2 = gd[0] * gd[0]; gd[0] = asf(split2(gd[0])); mn = u32x2{h ? 0u : pk2(mean, mean), 0}; }
          UNROLL for (int e = 0; e < 8; ++e) if (d >= 1 && (e == 0 || (d == 1 && e < 5) || (d == 2 && e != 1 && e != 3) || (d == 3 && (e == 4 || e == 6)))) asm volatile("" : "+v"(gd[e]));
        });
        SB();
        rb = mfma2(u32x2{asu(gd[0]), 0}, one2, z16);
        full_wait(IC<8>{});
        sfor<LA / 2>([&](auto T) INL { rdw(T); });
        SB();
        acc1 = mfma2(mn, csf, acc1);
        sfor<LA / 2>([&](auto T) INL { rdw(IC<T.v + LA / 2>{}); });
        asm volatile("" : "+v"(rb), "+v"(acc1), "+v"(rr2), "+v"(mn));
      }
      const int wm = w >> 1, wk = w & 1;
      unsigned hv, da[2][2], db[2];
      auto rdd = [&](auto T) INL {
        constexpr int t = T.v, sl = tile_at(21 + (t >> 3)) / SLOT;
        wf[(T.v + 224) % LA] = trd2((sl == 8 ? db[t & 1] : da[t & 1][sl >> 2]) + (sl & 3) * SLOT + 2048 * ((t >> 1) & 3), 1024);
      };
      int ho4[7], oo, zo; unsigned gx, gw; u32x2 xr[4];
      u32x4 zf[15];
      auto fetch = [&](auto C0, auto C1) INL {
        sfor<(C1.v > C0.v ? C1.v - C0.v : 0)>([&](auto K) INL {
          constexpr int f = C0.v + K.v, n = (f + 4) % 15;
          if (f >= 56) zf[n] = __builtin_amdgcn_raw_buffer_load_b128(rh, hv + (n & 1) * 2 * FS, n < 14 ? (n >> 1) * 4 * FS : oo, 16);
          else xf[f % 28] = __builtin_amdgcn_raw_buffer_load_b128(rh, hv + (f & 3) * FS, ho4[(f >> 2) % 7], 16);
        });
      };
      unsigned ge[2]; u64 gl[2];
      auto gate = [&](auto D, auto OFF, auto NEED, int i, unsigned at) INL {
        if (D.v == 3) ge[i] = *(volatile LDS unsigned*)lp(at + (OFF.v - MISC));
        if (D.v == 0 && OFF.v == C_GATE) cwait_p<OFF.v, NEED.v>(at, ge[i]);
        if (D.v == 1 && OFF.v != C_GATE) gl[i] = __builtin_amdgcn_ballot_w64(ge[i] < (unsigned)NEED.v);
        if (D.v == 0 && OFF.v != C_GATE && __builtin_expect(gl[i] != 0, 0)) cwait_i<OFF.v, NEED.v>(at);
      };
      {
        f32x16 acc[4]; f32x4 cs;
        unsigned xw; u64 xlate; u32x2 cx[4]; u32x4 ho, yd[2]; float ea[2]; f32x16 ya;
        auto fix = [&](int ps) { acc[ps] = mfma2(cx[ps], mn, acc[ps]); asm volatile("" : "+v"(acc[ps])); };
        auto epi = [&](auto K) INL {
          constexpr int ps = K.v / 28 % 4, e = K.v % 28 / 7, o = K.v % 7;
          if (o < 2) ea[o] = relu(acc[ps][2 * e + o]);
          else if (o < 4) ea[o - 2] *= acc[ps][6 + 2 * e + o];
          else if (o < 6) ea[o - 4] *= rr2;
          else ho[e] = pk2(ea[0], ea[1]);
          if (K.v % 28 == 27) st_wt(ho, rh, l16, so[ps]);
        };
        sfor<224>([&](auto T) INL {
          constexpr int t = T.v, hb = t / 112, R = t - (hb ? R1 : 80), N = t + LA;
          constexpr Sl o = slot_of(t);
          constexpr int ps = o.ps, u = 4 * o.i + o.s;
          SB();
          acc[ps] = mfma(wf[t % LA], xf[u], u ? acc[ps] : z16);
          if (ps == 0) cs = mfma16(sel, wf[t % LA], u ? cs : z4);
          asm volatile("" : "+v"(acc[ps]), "+v"(cs));
          SB();
          if (full_need(N)) full_wait(IC<full_need(N)>{});
          if constexpr (N < 224) rdw(IC<N>{}); else rdd(IC<N - 224>{});
          if (free_at(N)) cset<C_FREE>(cbw, free_at(N));
          SB();
          if (full_need(N + 6)) full_pre();
          if (full_need(N + 2)) full_cmp(full_need(N + 2));
          if (t == 1 || t == 2) {
            constexpr int e = (t - 1) & 1;
            UNROLL for (int q = 0; q < 4; ++q) yd[e][q] = pk2(acc1[8 * e + 2 * q] * rb[8 * e + 2 * q], acc1[8 * e + 2 * q + 1] * rb[8 * e + 2 * q + 1]);
            asm volatile("" : "+v"(yd[e]));
          }
          if (t == 2 || t == 3) {
            constexpr int e = t & 1;
            SB();
            ya = mfma(yd[e], yc[e], e ? ya : z16);
            SB();
            UNROLL for (int q = 0; q < 4; ++q) yd[e][q] = __builtin_amdgcn_update_dpp(0u, yd[e][q], 0xB1, 0xf, 0xf, true) ^ ysg;
            SB();
            ya = mfma(yd[e], ys[e], ya);
            asm volatile("" : "+v"(ya));
          }
          if (t == 5 || t == 6) {
            constexpr int e = (t - 5) & 1;
            UNROLL for (int q = 0; q < 4; ++q) ho[q] = pk2(ya[8 * e + 2 * q], ya[8 * e + 2 * q + 1]);
            st128(xs + 1024 * e, ho);
            if (e) cset<C_BN>(cbw, 1);
          }
          if (R == 0) { xs = CX + hb * 1024 + 128 * mi + ((l16 >> 2) & 124); cx[0] = u32x2{0, 0}; asm volatile("" : "+v"(xs), "+v"(cx[0])); }
          if (t == 148) { asm volatile("" : "+v"(l16)); hv = 128 * (4 * (l16 >> 9) + ((l16 >> 6) & 3)) + ((l16 >> 1) & 24); asm volatile("" : "+v"(hv)); }
          if (t == 152) { asm volatile("" : "+v"(l16)); gx = 32 * (((l16 >> 8) & 1) ^ ((l16 >> 6) & 2)); oo = 8128 * wk + 64; asm volatile("" : "+v"(gx), "+s"(oo)); }
          if (t == 156) { da[0][0] = (hv + gx) ^ oo; da[1][0] = da[0][0] ^ 64; oo = (2 * rt + wm) * NFG * FS; asm volatile("" : "+v"(da[0][0]), "+v"(da[1][0]), "+s"(oo)); }
          if (t == R1 + 1) {
            UNROLL for (int cn = 0; cn < 2; ++cn) { da[cn][1] = da[cn][0] + 4 * SLOT; db[cn] = da[cn][0] + 8 * SLOT; asm volatile("" : "+v"(da[cn][1]), "+v"(db[cn])); }
            hv = oo + l16; asm volatile("" : "+v"(hv));
          }
          if (t == 20) { oo = WS_HF + (4 * p + (mi >> 1)) * 256 + (2 * j + (mi & 1)) * 8; asm volatile("" : "+s"(oo)); }
          if (t == 24) { fp = ws + (unsigned)oo; rf = mkrsrc(fp, 8); asm volatile("" : "+s"(fp), "+s"(rf)); }
          if (t == R1 + 2) { rf = mkrsrc(fp + 512, 8); asm volatile("" : "+s"(rf)); }
          if (t == R1 + 3) { oo = (28 - wk) * FS; zo = (108 + wk) * FS; asm volatile("" : "+s"(oo), "+s"(zo)); }
          if (t == 28) { ho4[0] = (16 * rot + 8 * wk) * FS; asm volatile("" : "+s"(ho4[0])); }
          if (t >= 32 && t < 56 && t % 4 == 0) {
            constexpr int e = (t - 28) / 4 % 7;
            ho4[e] = ho4[(e + 6) % 7] + (ho4[(e + 6) % 7] < 96 * (int)FS ? 16 : -96) * (int)FS; asm volatile("" : "+s"(ho4[e]));
          }
          if (R == 9) { cx[0][0] = split2(cs[0]); asm volatile("" : "+v"(cx[0])); }
          if (R == 10) {
            asm volatile("ds_write2_b32 %0, %1, %1 offset1:128" :: "v"(xs), "v"(cx[0][0]) : "memory");
            cset<C_CX>(cbw, hb + 1);
            fix(0);
          }
          if (R == 13) xw = *(volatile LDS unsigned*)lp(cf + (C_CX - MISC));
          if (R == 15) xlate = __builtin_amdgcn_ballot_w64(xw < (unsigned)(hb + 1));
          if (R == 16) {
            if (__builtin_expect(xlate != 0, 0)) cwait_i<C_CX, hb + 1>(cb4);
            asm volatile("ds_read2_b32 %0, %3 offset0:32 offset1:64\n\tds_read2_b32 %1, %3 offset0:64 offset1:96\n\tds_read2_b32 %2, %3 offset0:96 offset1:128"
                         : "=&v"(cx[1]), "=&v"(cx[2]), "=&v"(cx[3]) : "v"(xs) : "memory");
          }
          if (R == 18) { asm volatile("s_waitcnt lgkmcnt(4)" : "+v"(cx[1]), "+v"(cx[2]), "+v"(cx[3])); fix(1); }
          if (R == 19 + 2 * hb) fix(3 - hb);
          if (R == 25) fix(2 + hb);
          constexpr int e0 = epi_done(t - 1), e1 = epi_done(t);
          sfor<e1 - e0>([&](auto K) INL { epi(IC<e0 + K.v>{}); });
          if (e1 > e0) {
            if (e1 % 7 > 0) asm volatile("" : "+v"(ea[0]));
            if (e1 % 7 > 1) asm volatile("" : "+v"(ea[1]));
            if (e1 / 7 > e0 / 7 && e1 % 28 >= 7) asm volatile("" : "+v"(ho[(e1 % 28 / 7 + 3) & 3]));
          }
          if (t == 144) UNROLL for (int e = 0; e < 4; ++e) { so[e] += 4 * FS; asm volatile("" : "+s"(so[e])); }
          if (t == HA_AT - 1) { tag = tg; asm volatile("" : "+v"(tag)); }
          if (t == HA_AT) { drain(); st_flag(tag, rf, l16); }
          if (t == 223) gate(IC<3>{}, IC<C_GATE>{}, IC<1>{}, 0, cf);
        });
        SB();
        sfor<28>([&](auto K) INL { epi(IC<196 + K.v>{}); });
      }
      {
        f32x16 acc4[2];
        gate(IC<0>{}, IC<C_GATE>{}, IC<1>{}, 0, cf);
        fetch(IC<0>{}, IC<PRE_H>{});
        asm volatile("s_waitcnt vmcnt(%0)" :: "n"(PRE_H) : "memory");
        st_flag(tag, rf, l16);
        unsigned ih, ia, ta[4]; u32x4 pp[4];
        sfor<141>([&](auto T) INL {
          constexpr int t = T.v, N = 224 + t + LA, k = t - 112, cn = k < 0 ? t & 1 : k >= 14;
          SB();
          acc4[cn] = mfma(wf[(t + 224) % LA], k < 0 ? xf[(t >> 1) % 28] : zf[(k < 14 ? k + 15 : k + 1) % 15], t >= 2 ? acc4[cn] : z16);
          asm volatile("" : "+v"(acc4[cn]));
          SB();
          if (N < 336) {
            if (full_need(N)) full_wait(IC<full_need(N)>{});
            rdd(IC<(N - 224) % 112>{});
            if (free_at(N)) cset<C_FREE>(cbw, free_at(N));
          } else if (N < 365) {
            constexpr int n = (N - 336) < 14 ? N - 336 : N - 350;
            if (N == 336) gate(IC<0>{}, IC<C_IF>{}, IC<1>{}, 1, cf);
            wf[N % LA] = lds128(n < 3 ? ih + n * 4096 : ia + SLOT + (n - 3) * 4096);
            if (N == 338) { ih ^= 1024; asm volatile("" : "+v"(ih)); }
            if (N == 349) { ia ^= 1024; asm volatile("" : "+v"(ia)); }
            if (N == 363) { ia &= ~2048u; asm volatile("" : "+v"(ia)); }
          }
          SB();
          if (full_need(N + 6)) full_pre();
          if (full_need(N + 2)) full_cmp(full_need(N + 2));
          if (t == G2 - 1 || t == GZ - 1) { hv += t < G2 ? 4 * FS : zo; asm volatile("" : "+v"(hv)); }
          gate(IC<G2 - t>{}, IC<C_GATE>{}, IC<2>{}, 0, cf);
          gate(IC<GZ - t>{}, IC<C_GZ>{}, IC<1>{}, 0, cf);
          fetch(IC<fetched(t - 1)>{}, IC<fetched(t)>{});
          if (N < 336) gate(IC<336 - N>{}, IC<C_IF>{}, IC<1>{}, 1, cf);
          if (t == 101) { ia = 1024 + 1024 * wk + l16; ih = ia + IFH; asm volatile("" : "+v"(ia), "+v"(ih)); }
          if (t == 108) { asm volatile("" : "+v"(l16)); gx = FDB + 4096 * wm + (l16 & 496) * 8 + ((l16 & 112) ^ (64 * wk)) + (l16 >> 9) * 8; asm volatile("" : "+v"(gx)); }
          if (t == 110 || t == 111) UNROLL for (int q = 2 * (t & 1); q < 2 * (t & 1) + 2; ++q) xr[q] = *(volatile LDS u32x2*)lp(gx ^ (16 * q));
          if (k >= 2 && k < 10) {
            constexpr int e = (k - 2) & 7;
            acc4[1][2 * e] += asf(xr[e >> 1][e & 1] << 16); acc4[1][2 * e + 1] += asf(xr[e >> 1][e & 1] & 0xffff0000u);
            asm volatile("" : "+v"(acc4[1]));
          }
          if (k == 13) { const int RT = 2 * rt + wm; oo = (2 * (RT & 31) + (RT >> 5)) * 896 + 128 * c7 + 64 * wk; asm volatile("" : "+s"(oo)); }
          if (k == 10) { xs = 4 * SLOT + 4096 * w + l16; gw = MISC + 4 * (w ^ 1); asm volatile("" : "+v"(xs), "+v"(gw)); }
          if (k == 11) { asm volatile("" : "+v"(l16)); gx = (l16 >> 6) * 57344 + (((l16 >> 4) ^ l16) & 48); asm volatile("" : "+v"(gx)); }
          if (k == 18) { ih = FDB + 8192 + 2048 * w + l16; asm volatile("" : "+v"(ih)); }
          if (k == 19) { ta[0] = FDB + 8192 + 2048 * w + (l16 & 496) * 4 + ((l16 >> 2) & 48) + (l16 >> 9) * 8; asm volatile("" : "+v"(ta[0])); }
          if (k == 20) UNROLL for (int q = 1; q < 4; ++q) { ta[q] = ta[0] ^ (16 * q); asm volatile("" : "+v"(ta[q])); }
          if (k == 12) { rh = mkrsrc(out, 2048 * 896); asm volatile("" : "+s"(rh)); }
          if (k == EXW || k == EXW + 1) {
            UNROLL for (int q = 2 * (k - EXW); q < 2 * (k - EXW) + 2; ++q)
              st128(xs + 1024 * q, __builtin_bit_cast(u32x4, f32x4{acc4[0][4 * q], acc4[0][4 * q + 1], acc4[0][4 * q + 2], acc4[0][4 * q + 3]}));
            if (k > EXW) { cset<C_EX>(cbw, 1); xs ^= 4096; asm volatile("" : "+v"(xs)); }
          }
          gate(IC<EXR - k>{}, IC<C_EX>{}, IC<1>{}, 0, gw);
          if (k == EXR) UNROLL for (int q = 0; q < 4; ++q) pp[q] = *(volatile LDS u32x4*)lp(xs + 1024 * q);
        });
        SB();
        u32x4 o2[2];
        {
          UNROLL for (int q = 0; q < 4; ++q)
            *(volatile LDS u32x2*)lp(ta[q]) = u32x2{pk2(acc4[1][4 * q] + asf(pp[q][0]), acc4[1][4 * q + 1] + asf(pp[q][1])), pk2(acc4[1][4 * q + 2] + asf(pp[q][2]), acc4[1][4 * q + 3] + asf(pp[q][3]))};
          UNROLL for (int i = 0; i < 2; ++i) o2[i] = *(volatile LDS u32x4*)lp(ih + 1024 * i);
          UNROLL for (int i = 0; i < 2; ++i) __builtin_amdgcn_raw_buffer_store_b128(o2[i], rh, gx, oo + 16 * 57344 * i, 16);
        }
        asm volatile("s_nop 1" :: "v"(o2[0]), "v"(o2[1]) : "memory");
      }
#undef cb4
    } else {
      u32x4 fr[29];
      {
        const float col = (float)(64 * c7 + 32 * lw + m), k8 = 2.f * __builtin_amdgcn_cosf(turn(8.f * col, 0.f));
        float c[2][8];
        UNROLL for (int i = 0; i < 8; ++i) {
          const float ph = turn((float)(8 * (i >> 2) + (i & 3) + 4 * h) * col, 0.f);
          c[i >> 2][2 * (i & 3)] = __builtin_amdgcn_cosf(ph); c[i >> 2][2 * (i & 3) + 1] = -__builtin_amdgcn_sinf(ph);
        }
        UNROLL for (int q = 0; q < 29; ++q) {
          UNROLL for (int e = 0; e < 4; ++e) fr[q][e] = q < 28 ? pk2(c[q & 1][2 * e], c[q & 1][2 * e + 1]) : pk2(0.f, c[q & 1][2 * e]);
          if (q < 27) UNROLL for (int e = 0; e < 8; ++e) c[q & 1][e] = __builtin_fmaf(k8, c[~q & 1][e], -c[q & 1][e]);
          asm volatile("" : "+v"(fr[q]));
        }
      }
      auto put = [&](auto Q0, auto Q1) INL {
        unsigned ia = (Q0.v ? SLOT - 12288 : IFH) + 1024 * lw + lane * 16; asm volatile("" : "+v"(ia));
        UNROLL for (int q = Q0.v; q < Q1.v; ++q) {
          if (q == 0) cwaitl<C_FREE>(cb4, 7);
          if (q == 6) cwaitl<C_FREE>(cb4, 26);
          if ((q & 7) == 6) cwaits<C_FREE>(cb4, 27 + (q >> 3));
          st128(ia + q * 2048, fr[q]);
          SB();
        }
      };
      __builtin_amdgcn_s_setprio(HPRIO);
      {
        tag += TAG_K;
        cwaits<C_BN>(cb4, 1);
        u32x4 v[4];
        const rsrc_t rx = mkrsrc(ws + WS_XD, XD_BYTES);
        UNROLL for (int n = 0; n < 4; ++n) v[n] = lds128(6 * SLOT + n * 4096 + lw * 1024 + lane * 16);
        cset<C_SD>(cbw, 1);
        UNROLL for (int i = 0; i < 4; ++i)
          st_wt(u32x4{__builtin_amdgcn_perm(v[1][i], v[0][i], 0x05040100u), __builtin_amdgcn_perm(v[3][i], v[2][i], 0x05040100u),
                      __builtin_amdgcn_perm(v[1][i], v[0][i], 0x07060302u), __builtin_amdgcn_perm(v[3][i], v[2][i], 0x07060302u)},
                rx, h * 16384 + m * 16, (16 * j + 8 * lw + 4 * (i >> 1) + (i & 1)) * 8192 + p * 512);
        drain();
        if (lane == 0) gstore64(ws + WS_XF + j * 256 + (2 * p + lw) * 8, tag);
      }
      put(IC<0>{}, IC<6>{});
      u32x4 xv[4];
      UNROLL for (int i = 0; i < 4; ++i) {
        const int RT = 2 * rt + lw;
        xv[i] = __builtin_amdgcn_raw_buffer_load_b128(rsx, (lane >> 3) * 57344 + 16 * ((lane ^ (lane >> 3)) & 7) + 3072, (2 * (RT & 31) + (RT >> 5)) * 896 + 128 * c7 + 8 * 57344 * i, 0);
      }
      if (w == 6) {
        cwaitl<C_FREE>(cb4, PSZ);
        poll_set<C_GZ>(ws + WS_ZF + xcd * 256, 8 * min(lane, 28), tag, cb4, 1);
        if (b == 0 && lane == 0) gstore64(ws + WS_E, tag - TAG_K + 1);
      } else {
        const char* hf = ws + WS_HF + ((rt >> 1) * 4 + (rt & 1)) * 256;
        cwaitl<C_FREE>(cb4, PSTART);
        __builtin_amdgcn_s_setprio(PPRIO);
        poll_set<C_GATE>(hf, 8 * min(lane, 27), tag, cb4, 1);
        cwaitl<C_FREE>(cb4, PS2);
        poll_set<C_GATE>(hf + 512, 8 * min(lane, 27), tag, cb4, 2);
      }
      put(IC<6>{}, IC<29>{});
      cwaits<C_FREE>(cb4, 31);
      UNROLL for (int i = 0; i < 4; ++i) st128(FDB + 4096 * lw + 1024 * i + lane * 16, xv[i]);
      cset<C_IF>(cbw, 1);
    }
  }
}

#if defined(__HIP_DEVICE_COMPILE__)
#pragma clang attribute pop
#endif

extern "C" void kernel_launch(void* const* d_in, const int* in_sizes, int n_in, void* d_out, int out_size, void* d_ws, size_t ws_size,
                              hipStream_t stream) {
  (void)in_sizes; (void)n_in; (void)out_size; (void)ws_size;
  (void)hipFuncSetAttribute((const void*)fused_kernel, hipFuncAttributeMaxDynamicSharedMemorySize, LDS_BYTES);
  hipLaunchKernelGGL(fused_kernel, dim3(256), dim3(512), LDS_BYTES, stream, (const char*)d_in[0], (const char*)d_in[1], (const char*)d_in[2],
                     (const char*)d_in[4], (const char*)d_in[5], (const char*)d_in[6], (char*)d_out, (char*)d_ws);
}
```
